```python
import torch

def _softmax(x, axis=-1):
    return torch.softmax(x, dim=axis)
def _gelu(x):
    return torch.nn.functional.gelu(x, approximate="tanh")
def _silu(x):
    return torch.nn.functional.silu(x)
def _sigmoid(x):
    return torch.sigmoid(x)
def _layernorm(x, axis=-1, eps=1e-5):
    m = x.mean(dim=axis, keepdim=True)
    v = x.var(dim=axis, keepdim=True, unbiased=False)
    return (x - m) / torch.sqrt(v + eps)
def _scatter_add(tgt, idx, src, axis=0):
    return tgt.clone().index_add_(axis, idx, src)
def _segment_sum(src, seg, n):
    out = torch.zeros((n,) + src.shape[1:], dtype=src.dtype)
    return out.index_add_(0, seg, src)
def _pad(x, pw):
    flat = tuple(v for pair in reversed(pw) for v in pair)
    return torch.nn.functional.pad(x, flat)

kernel_name = 'parallel_sum__mha_block__dense_activation_gelu__prompt'

def setup_inputs():

    _w = (lambda a: (lambda t: t.to(torch.bfloat16) if t.is_floating_point() else t)(torch.as_tensor(a)))
    def _A_setup():
        B, S, D, H = 1, 2048, 512, 32
        return {
            "x": _w((lambda *s: torch.randn(*s, dtype=torch.bfloat16) * 0.05)(B, S, D)),
            "wqkv": _w((lambda *s: torch.randn(*s, dtype=torch.bfloat16) * 0.05)(D, 3 * D)),
            "wo": _w((lambda *s: torch.randn(*s, dtype=torch.bfloat16) * 0.05)(D, D)),
        }
    _A_in = _A_setup()

    def _B_setup():
        B, S, D, F = 1, 2048, 512, 1792
        return {
            "x": _w((lambda *s: torch.randn(*s, dtype=torch.bfloat16) * 0.05)(B, S, D)),
            "w1": _w((lambda *s: torch.randn(*s, dtype=torch.bfloat16) * 0.8)(D, F) * (D ** -0.5)),
            "b1": _w((lambda *s: torch.randn(*s, dtype=torch.bfloat16) * 12.8)(F) * 0.01),
            "w2": _w((lambda *s: torch.randn(*s, dtype=torch.bfloat16) * 0.8)(F, D) * (F ** -0.5)),
            "b2": _w((lambda *s: torch.randn(*s, dtype=torch.bfloat16) * 12.8)(D) * 0.01),
        }
    _B_in = _B_setup()

    return {
        "A_x": _A_in["x"],
        "A_wqkv": _A_in["wqkv"],
        "A_wo": _A_in["wo"],
        "B_x": _B_in["x"],
        "B_w1": _B_in["w1"],
        "B_b1": _B_in["b1"],
        "B_w2": _B_in["w2"],
        "B_b2": _B_in["b2"],
    }

def reference(A_x, A_wqkv, A_wo, B_x, B_w1, B_b1, B_w2, B_b2):
    def _A_ref(x, wqkv, wo):
        B, S, D = x.shape
        H = 32
        Dh = D // H
        qkv = (lambda x, *d: x.permute(*d))((x @ wqkv).reshape(B, S, 3, H, Dh), 2, 0, 3, 1, 4)
        q, k, v = qkv[0], qkv[1], qkv[2]
        scores = torch.einsum("bhqd,bhkd->bhqk", q, k) * (Dh ** -0.5)
        p = _softmax(scores, axis=-1)
        o = torch.einsum("bhqk,bhkd->bhqd", p, v)
        return (lambda x, *d: x.permute(*d))(o, 0, 2, 1, 3).reshape(B, S, D) @ wo

    def _B_ref(x, w1, b1, w2, b2):
        h = torch.einsum("bsd,df->bsf", x, w1) + b1
        h = _gelu(h)
        return torch.einsum("bsf,fd->bsd", h, w2) + b2

    x = A_x
    xn = _layernorm(x, axis=-1)
    return x + _A_ref(xn, A_wqkv, A_wo) + _B_ref(xn, B_w1, B_b1, B_w2, B_b2)
```

```cpp
#include <hip/hip_runtime.h>
#include <hip/hip_bf16.h>
#include <stdint.h>

typedef __attribute__((ext_vector_type(8))) short bf16x8;
typedef __attribute__((ext_vector_type(4))) short bf16x4;
typedef __attribute__((ext_vector_type(4))) float f32x4;
typedef __attribute__((ext_vector_type(16))) float f32x16;
typedef __attribute__((ext_vector_type(4))) unsigned int u32x4;
typedef __attribute__((ext_vector_type(2))) unsigned int u32x2;
typedef __attribute__((address_space(3))) bf16x4* lds_b4_ptr;

constexpr int S_ = 2048, D_ = 512, H_ = 32, F_ = 1792;

constexpr size_t WS_Q = 0;
constexpr size_t WS_K = WS_Q + (size_t)H_ * S_ * 16 * 2;
constexpr size_t WS_V = WS_K + (size_t)H_ * S_ * 16 * 2;
constexpr size_t WS_H = WS_V + (size_t)H_ * S_ * 16 * 2;
constexpr size_t WS_Y = WS_H + (size_t)S_ * F_ * 2;
constexpr size_t WS_O = WS_Y + (size_t)S_ * D_ * 4;

__device__ __forceinline__ unsigned short f2bf(float f) {
  __hip_bfloat16 b = __float2bfloat16(f);
  return *reinterpret_cast<unsigned short*>(&b);
}
__device__ __forceinline__ float bf2f(unsigned short u) { return __uint_as_float(((unsigned)u) << 16); }
__device__ __forceinline__ unsigned pack2(float a, float b) { return (unsigned)f2bf(a) | ((unsigned)f2bf(b) << 16); }

template <int BM, int BN, int WM, int WN>
struct GemmCfg {
  static constexpr int NT = WM * WN * 64;
  static constexpr int BK = 64;
  static constexpr int B_STRIDE = BN * 2 + 32;
  static constexpr int A_BYTES = BM * 128;
  static constexpr int B_BYTES = BK * B_STRIDE;
  static constexpr int STAGE = A_BYTES + B_BYTES;
  static constexpr int TM = BM / WM / 16;
  static constexpr int TN = BN / WN / 16;
  static constexpr int A_LD = (BM * 8) / NT;
  static constexpr int B_LD = (BK * BN / 8) / NT;
};

template <int BM, int BN, int WM, int WN, bool SWAP, bool COLSUM>
__device__ __forceinline__ void gemm_mainloop(const unsigned short* __restrict__ A, int lda,
                                              const unsigned short* __restrict__ B, int ldb, int K,
                                              char* smem,
                                              f32x4 (&acc)[GemmCfg<BM, BN, WM, WN>::TM][GemmCfg<BM, BN, WM, WN>::TN],
                                              f32x4 (&cs)[GemmCfg<BM, BN, WM, WN>::TN]) {
  using C = GemmCfg<BM, BN, WM, WN>;
  const int tid = threadIdx.x, lane = tid & 63, wid = tid >> 6;
  const int wm = wid / WN, wn = wid % WN;
  u32x4 ra[C::A_LD], rb[C::B_LD];

  auto gload = [&](int kt) {
#pragma unroll
    for (int i = 0; i < C::A_LD; ++i) {
      int c = tid + i * C::NT, row = c >> 3, ch = c & 7;
      ra[i] = *reinterpret_cast<const u32x4*>(A + (size_t)row * lda + kt * 64 + ch * 8);
    }
#pragma unroll
    for (int i = 0; i < C::B_LD; ++i) {
      int c = tid + i * C::NT, row = c / (BN / 8), ch = c % (BN / 8);
      rb[i] = *reinterpret_cast<const u32x4*>(B + (size_t)(kt * 64 + row) * ldb + ch * 8);
    }
  };
  auto swrite = [&](int buf) {
    char* base = smem + buf * C::STAGE;
#pragma unroll
    for (int i = 0; i < C::A_LD; ++i) {
      int c = tid + i * C::NT, row = c >> 3, ch = c & 7;
      *reinterpret_cast<u32x4*>(base + row * 128 + ((ch ^ (row & 7)) << 4)) = ra[i];
    }
#pragma unroll
    for (int i = 0; i < C::B_LD; ++i) {
      int c = tid + i * C::NT, row = c / (BN / 8), ch = c % (BN / 8);
      *reinterpret_cast<u32x4*>(base + C::A_BYTES + row * C::B_STRIDE + ch * 16) = rb[i];
    }
  };

  const int nk = K / 64;
  gload(0);
  swrite(0);
  __syncthreads();
  const int g = lane >> 4, q = (lane & 15) >> 2, p = lane & 3;
  bf16x8 ones;
#pragma unroll
  for (int j = 0; j < 8; ++j) ones[j] = (short)0x3F80;

  for (int kt = 0; kt < nk; ++kt) {
    if (kt + 1 < nk) gload(kt + 1);
    const char* base = smem + (kt & 1) * C::STAGE;
#pragma unroll
    for (int kk = 0; kk < 2; ++kk) {
      bf16x8 af[C::TM], bf[C::TN];
#pragma unroll
      for (int i = 0; i < C::TM; ++i) {
        int row = wm * (BM / WM) + i * 16 + (lane & 15);
        int ch = kk * 4 + g;
        af[i] = *reinterpret_cast<const bf16x8*>(base + row * 128 + ((ch ^ (row & 7)) << 4));
      }
#pragma unroll
      for (int j = 0; j < C::TN; ++j) {
        int col = wn * (BN / WN) + j * 16 + 4 * p;
        const char* a0 = base + C::A_BYTES + (kk * 32 + 8 * g + q) * C::B_STRIDE + col * 2;
        bf16x4 lo = __builtin_amdgcn_ds_read_tr16_b64_v4i16((lds_b4_ptr)(a0));
        bf16x4 hi = __builtin_amdgcn_ds_read_tr16_b64_v4i16((lds_b4_ptr)(a0 + 4 * C::B_STRIDE));
        bf[j] = __builtin_shufflevector(lo, hi, 0, 1, 2, 3, 4, 5, 6, 7);
      }
#pragma unroll
      for (int i = 0; i < C::TM; ++i)
#pragma unroll
        for (int j = 0; j < C::TN; ++j) {
          if (SWAP)
            acc[i][j] = __builtin_amdgcn_mfma_f32_16x16x32_bf16(bf[j], af[i], acc[i][j], 0, 0, 0);
          else
            acc[i][j] = __builtin_amdgcn_mfma_f32_16x16x32_bf16(af[i], bf[j], acc[i][j], 0, 0, 0);
        }
      if (COLSUM) {
        if (((kt * 2 + kk) % WM) == wm) {
#pragma unroll
          for (int j = 0; j < C::TN; ++j) cs[j] = __builtin_amdgcn_mfma_f32_16x16x32_bf16(ones, bf[j], cs[j], 0, 0, 0);
        }
      }
    }
    if (kt + 1 < nk) swrite((kt + 1) & 1);
    __syncthreads();
  }
}

__device__ __forceinline__ float gelu_tanh(float x) {
  float u = 0.7978845608028654f * (x + 0.044715f * x * x * x);
  float e = __expf(2.f * u);
  float t = 1.f - 2.f / (e + 1.f);
  return 0.5f * x * (1.f + t);
}

constexpr int K1_BM = 128, K1_BN = 256, K1_WM = 2, K1_WN = 4;
using K1C = GemmCfg<K1_BM, K1_BN, K1_WM, K1_WN>;

__global__ __launch_bounds__(512) void k1_kernel(const unsigned short* __restrict__ x,
                                                 const unsigned short* __restrict__ wqkv,
                                                 const unsigned short* __restrict__ w1,
                                                 const unsigned short* __restrict__ b1, char* __restrict__ ws) {
  extern __shared__ __attribute__((aligned(16))) char smem[];
  __shared__ float s_mean[K1_BM], s_rstd[K1_BM], s_cs[K1_WM][K1_BN];
  const int tid = threadIdx.x, lane = tid & 63, wid = tid >> 6;
  const int wm = wid / K1_WN, wn = wid % K1_WN;
  const int mt = blockIdx.x / 13, nt = blockIdx.x % 13;
  const int m0 = mt * K1_BM;
  const unsigned short* A = x + (size_t)m0 * D_;

  {
    int row = tid >> 2, part = tid & 3;
    const unsigned short* rp = A + (size_t)row * D_ + part * 128;
    float s = 0.f, ss = 0.f;
#pragma unroll
    for (int i = 0; i < 16; ++i) {
      u32x4 v = *reinterpret_cast<const u32x4*>(rp + i * 8);
#pragma unroll
      for (int j = 0; j < 4; ++j) {
        float a = __uint_as_float(v[j] << 16), b = __uint_as_float(v[j] & 0xffff0000u);
        s += a + b;
        ss += a * a + b * b;
      }
    }
    s += __shfl_xor(s, 1); s += __shfl_xor(s, 2);
    ss += __shfl_xor(ss, 1); ss += __shfl_xor(ss, 2);
    float mean = s * (1.f / D_);
    float var = fmaxf(ss * (1.f / D_) - mean * mean, 0.f);
    if (part == 0) { s_mean[row] = mean; s_rstd[row] = rsqrtf(var + 1e-5f); }
  }

  f32x4 acc[K1C::TM][K1C::TN];
  f32x4 cs[K1C::TN];
#pragma unroll
  for (int i = 0; i < K1C::TM; ++i)
#pragma unroll
    for (int j = 0; j < K1C::TN; ++j) acc[i][j] = f32x4{0, 0, 0, 0};
#pragma unroll
  for (int j = 0; j < K1C::TN; ++j) cs[j] = f32x4{0, 0, 0, 0};

  const bool is_v = (nt == 4 || nt == 5);
  if (nt < 6) {
    const unsigned short* B = wqkv + nt * K1_BN;
    if (is_v)
      gemm_mainloop<K1_BM, K1_BN, K1_WM, K1_WN, false, true>(A, D_, B, 3 * D_, D_, smem, acc, cs);
    else
      gemm_mainloop<K1_BM, K1_BN, K1_WM, K1_WN, true, true>(A, D_, B, 3 * D_, D_, smem, acc, cs);
  } else {
    const unsigned short* B = w1 + (nt - 6) * K1_BN;
    gemm_mainloop<K1_BM, K1_BN, K1_WM, K1_WN, true, true>(A, D_, B, F_, D_, smem, acc, cs);
  }
  if (lane < 16) {
#pragma unroll
    for (int j = 0; j < K1C::TN; ++j) s_cs[wm][wn * 64 + j * 16 + lane] = cs[j][0];
  }
  __syncthreads();

#pragma unroll
  for (int i = 0; i < K1C::TM; ++i) {
#pragma unroll
    for (int j = 0; j < K1C::TN; ++j) {
      const int nloc = wn * 64 + j * 16;
      const int mloc = wm * 64 + i * 16;
      if (is_v) {
        int n = nloc + (lane & 15);
        float csn = s_cs[0][n] + s_cs[1][n];
        float v[4];
#pragma unroll
        for (int r = 0; r < 4; ++r) {
          int m = mloc + 4 * (lane >> 4) + r;
          v[r] = s_rstd[m] * (acc[i][j][r] - s_mean[m] * csn);
        }
        int head = (nt - 4) * 16 + (nloc >> 4);
        int key = m0 + mloc + 4 * (lane >> 4);
        int grp = key >> 4, kk = key & 15;
        int hh = (kk >> 2) & 1, jb = (kk >> 3) * 4;
        unsigned short* dst = reinterpret_cast<unsigned short*>(ws + WS_V) + (size_t)head * S_ * 16 +
                              ((size_t)(grp * 2 + hh) * 16 + (lane & 15)) * 8 + jb;
        *reinterpret_cast<u32x2*>(dst) = u32x2{pack2(v[0], v[1]), pack2(v[2], v[3])};
      } else {
        int m = mloc + (lane & 15);
        float mean = s_mean[m], rstd = s_rstd[m];
        float v[4];
#pragma unroll
        for (int r = 0; r < 4; ++r) {
          int n = nloc + 4 * (lane >> 4) + r;
          float csn = s_cs[0][n] + s_cs[1][n];
          v[r] = rstd * (acc[i][j][r] - mean * csn);
        }
        if (nt < 4) {
          const bool isq = nt < 2;
          if (isq) {
#pragma unroll
            for (int r = 0; r < 4; ++r) v[r] *= 0.25f * 1.4426950408889634f;
          }
          int head = (nt & 1) * 16 + (nloc >> 4);
          unsigned short* dst = reinterpret_cast<unsigned short*>(ws + (isq ? WS_Q : WS_K)) +
                                ((size_t)head * S_ + m0 + m) * 16 + 4 * (lane >> 4);
          *reinterpret_cast<u32x2*>(dst) = u32x2{pack2(v[0], v[1]), pack2(v[2], v[3])};
        } else {
          int n = (nt - 6) * K1_BN + nloc + 4 * (lane >> 4);
#pragma unroll
          for (int r = 0; r < 4; ++r) v[r] = gelu_tanh(v[r] + bf2f(b1[n + r]));
          unsigned short* dst = reinterpret_cast<unsigned short*>(ws + WS_H) + (size_t)(m0 + m) * F_ + n;
          *reinterpret_cast<u32x2*>(dst) = u32x2{pack2(v[0], v[1]), pack2(v[2], v[3])};
        }
      }
    }
  }
}

constexpr int F2_BM = 64, F2_BN = 64, F2_WM = 4, F2_WN = 2;
using F2C = GemmCfg<F2_BM, F2_BN, F2_WM, F2_WN>;

__device__ __forceinline__ void pack_p(const f32x16& p, bf16x8& p0, bf16x8& p1) {
  unsigned w[8];
#pragma unroll
  for (int i = 0; i < 8; ++i) {
    asm("v_cvt_pk_bf16_f32 %0, %1, %2" : "=v"(w[i]) : "v"(p[2 * i]), "v"(p[2 * i + 1]));
  }
  p0 = __builtin_bit_cast(bf16x8, u32x4{w[0], w[1], w[2], w[3]});
  p1 = __builtin_bit_cast(bf16x8, u32x4{w[4], w[5], w[6], w[7]});
}

__global__ __launch_bounds__(512) void k2_kernel(char* __restrict__ ws, const unsigned short* __restrict__ w2) {
  extern __shared__ __attribute__((aligned(16))) char smem[];
  const int tid = threadIdx.x, lane = tid & 63, wid = tid >> 6;
  if (blockIdx.x >= 256) {
    const int b = blockIdx.x - 256;
    const int mt = b >> 3, nt = b & 7;
    const int wm = wid / F2_WN, wn = wid % F2_WN;
    f32x4 acc[F2C::TM][F2C::TN];
    f32x4 cs[F2C::TN];
#pragma unroll
    for (int i = 0; i < F2C::TM; ++i)
#pragma unroll
      for (int j = 0; j < F2C::TN; ++j) acc[i][j] = f32x4{0, 0, 0, 0};
    const unsigned short* A = reinterpret_cast<const unsigned short*>(ws + WS_H) + (size_t)mt * F2_BM * F_;
    gemm_mainloop<F2_BM, F2_BN, F2_WM, F2_WN, true, false>(A, F_, w2 + nt * F2_BN, D_, F_, smem, acc, cs);
    float* y = reinterpret_cast<float*>(ws + WS_Y);
#pragma unroll
    for (int i = 0; i < F2C::TM; ++i)
#pragma unroll
      for (int j = 0; j < F2C::TN; ++j) {
        int m = mt * F2_BM + wm * (F2_BM / F2_WM) + i * 16 + (lane & 15);
        int n = nt * F2_BN + wn * (F2_BN / F2_WN) + j * 16 + 4 * (lane >> 4);
        *reinterpret_cast<f32x4*>(y + (size_t)m * D_ + n) = acc[i][j];
      }
    return;
  }
  const int head = blockIdx.x >> 3, qblk = blockIdx.x & 7;
  char* sK = smem;
  char* sV = smem + 65536;
  {
    const u32x4* gK = reinterpret_cast<const u32x4*>(ws + WS_K + (size_t)head * 65536);
    const u32x4* gV = reinterpret_cast<const u32x4*>(ws + WS_V + (size_t)head * 65536);
#pragma unroll
    for (int i = 0; i < 8; ++i) {
      reinterpret_cast<u32x4*>(sK)[tid + i * 512] = gK[tid + i * 512];
      reinterpret_cast<u32x4*>(sV)[tid + i * 512] = gV[tid + i * 512];
    }
  }
  const int q0 = qblk * 256 + wid * 32;
  const int hh = lane >> 5, r31 = lane & 31;
  bf16x8 qf = *reinterpret_cast<const bf16x8*>(ws + WS_Q + ((size_t)head * S_ + q0 + r31) * 32 + hh * 16);
  __syncthreads();

  bf16x8 vconst;
#pragma unroll
  for (int j = 0; j < 8; ++j) vconst[j] = (r31 == 16) ? (short)0x3F80 : (short)0;

  auto kfrag = [&](int tile) { return *reinterpret_cast<const bf16x8*>(sK + (tile * 32 + r31) * 32 + hh * 16); };
  auto vfrag = [&](int grp) {
    bf16x8 v = vconst;
    if (r31 < 16) v = *reinterpret_cast<const bf16x8*>(sV + ((grp * 2 + hh) * 16 + r31) * 16);
    return v;
  };

  f32x16 zero16;
#pragma unroll
  for (int i = 0; i < 16; ++i) zero16[i] = 0.f;
  f32x16 negm;
  {
    f32x16 s = __builtin_amdgcn_mfma_f32_32x32x16_bf16(kfrag(0), qf, zero16, 0, 0, 0);
    float m = s[0];
#pragma unroll
    for (int i = 1; i < 16; ++i) m = fmaxf(m, s[i]);
    m = fmaxf(m, __shfl_xor(m, 32));
#pragma unroll
    for (int i = 0; i < 16; ++i) negm[i] = -m;
  }
  f32x16 o = zero16;
  for (int t = 0; t < 64; ++t) {
    f32x16 s = __builtin_amdgcn_mfma_f32_32x32x16_bf16(kfrag(t), qf, negm, 0, 0, 0);
#pragma unroll
    for (int i = 0; i < 16; ++i) s[i] = __builtin_amdgcn_exp2f(s[i]);
    bf16x8 p0, p1;
    pack_p(s, p0, p1);
    o = __builtin_amdgcn_mfma_f32_32x32x16_bf16(vfrag(2 * t), p0, o, 0, 0, 0);
    o = __builtin_amdgcn_mfma_f32_32x32x16_bf16(vfrag(2 * t + 1), p1, o, 0, 0, 0);
  }
  float l = o[8];
  l = __shfl(l, r31);
  bool bad = !(l < 1.8446744073709552e19f);
  if (__any(bad)) {
    float m = -INFINITY;
    float lacc = 0.f;
    o = zero16;
    for (int t = 0; t < 64; ++t) {
      f32x16 s = __builtin_amdgcn_mfma_f32_32x32x16_bf16(kfrag(t), qf, zero16, 0, 0, 0);
      float tm = s[0];
#pragma unroll
      for (int i = 1; i < 16; ++i) tm = fmaxf(tm, s[i]);
      tm = fmaxf(tm, __shfl_xor(tm, 32));
      float mn = fmaxf(m, tm);
      float alpha = __builtin_amdgcn_exp2f(m - mn);
      m = mn;
#pragma unroll
      for (int i = 0; i < 16; ++i) { s[i] = __builtin_amdgcn_exp2f(s[i] - m); o[i] *= alpha; }
      bf16x8 p0, p1;
      pack_p(s, p0, p1);
      o = __builtin_amdgcn_mfma_f32_32x32x16_bf16(vfrag(2 * t), p0, o, 0, 0, 0);
      o = __builtin_amdgcn_mfma_f32_32x32x16_bf16(vfrag(2 * t + 1), p1, o, 0, 0, 0);
    }
    lacc = o[8];
    l = __shfl(lacc, r31);
  }
  float inv = 1.f / l;
  unsigned short* dst = reinterpret_cast<unsigned short*>(ws + WS_O) + (size_t)(q0 + r31) * D_ + head * 16 + 4 * hh;
  *reinterpret_cast<u32x2*>(dst) = u32x2{pack2(o[0] * inv, o[1] * inv), pack2(o[2] * inv, o[3] * inv)};
  *reinterpret_cast<u32x2*>(dst + 8) = u32x2{pack2(o[4] * inv, o[5] * inv), pack2(o[6] * inv, o[7] * inv)};
}

constexpr int K3_BM = 64, K3_BN = 64, K3_WM = 2, K3_WN = 2;
using K3C = GemmCfg<K3_BM, K3_BN, K3_WM, K3_WN>;

__global__ __launch_bounds__(256) void k3_kernel(const unsigned short* __restrict__ x,
                                                 const unsigned short* __restrict__ wo,
                                                 const unsigned short* __restrict__ b2, const char* __restrict__ ws,
                                                 unsigned short* __restrict__ out) {
  extern __shared__ __attribute__((aligned(16))) char smem[];
  const int tid = threadIdx.x, lane = tid & 63, wid = tid >> 6;
  const int mt = blockIdx.x >> 3, nt = blockIdx.x & 7;
  const int wm = wid / K3_WN, wn = wid % K3_WN;
  f32x4 acc[K3C::TM][K3C::TN];
  f32x4 cs[K3C::TN];
#pragma unroll
  for (int i = 0; i < K3C::TM; ++i)
#pragma unroll
    for (int j = 0; j < K3C::TN; ++j) acc[i][j] = f32x4{0, 0, 0, 0};
  const unsigned short* A = reinterpret_cast<const unsigned short*>(ws + WS_O) + (size_t)mt * K3_BM * D_;
  gemm_mainloop<K3_BM, K3_BN, K3_WM, K3_WN, true, false>(A, D_, wo + nt * K3_BN, D_, D_, smem, acc, cs);
  const float* y = reinterpret_cast<const float*>(ws + WS_Y);
#pragma unroll
  for (int i = 0; i < K3C::TM; ++i)
#pragma unroll
    for (int j = 0; j < K3C::TN; ++j) {
      int m = mt * K3_BM + wm * (K3_BM / K3_WM) + i * 16 + (lane & 15);
      int n = nt * K3_BN + wn * (K3_BN / K3_WN) + j * 16 + 4 * (lane >> 4);
      f32x4 yv = *reinterpret_cast<const f32x4*>(y + (size_t)m * D_ + n);
      u32x2 xv = *reinterpret_cast<const u32x2*>(x + (size_t)m * D_ + n);
      u32x2 bv = *reinterpret_cast<const u32x2*>(b2 + n);
      float r0 = acc[i][j][0] + yv[0] + __uint_as_float(xv[0] << 16) + __uint_as_float(bv[0] << 16);
      float r1 = acc[i][j][1] + yv[1] + __uint_as_float(xv[0] & 0xffff0000u) + __uint_as_float(bv[0] & 0xffff0000u);
      float r2 = acc[i][j][2] + yv[2] + __uint_as_float(xv[1] << 16) + __uint_as_float(bv[1] << 16);
      float r3 = acc[i][j][3] + yv[3] + __uint_as_float(xv[1] & 0xffff0000u) + __uint_as_float(bv[1] & 0xffff0000u);
      *reinterpret_cast<u32x2*>(out + (size_t)m * D_ + n) = u32x2{pack2(r0, r1), pack2(r2, r3)};
    }
}

extern "C" void kernel_launch(void* const* d_in, const int* in_sizes, int n_in, void* d_out, int out_size,
                              void* d_ws, size_t ws_size, hipStream_t stream) {
  const unsigned short* x = (const unsigned short*)d_in[0];
  const unsigned short* wqkv = (const unsigned short*)d_in[1];
  const unsigned short* wo = (const unsigned short*)d_in[2];
  const unsigned short* w1 = (const unsigned short*)d_in[4];
  const unsigned short* b1 = (const unsigned short*)d_in[5];
  const unsigned short* w2 = (const unsigned short*)d_in[6];
  const unsigned short* b2 = (const unsigned short*)d_in[7];
  char* ws = (char*)d_ws;
  unsigned short* out = (unsigned short*)d_out;

  (void)hipFuncSetAttribute((const void*)k1_kernel, hipFuncAttributeMaxDynamicSharedMemorySize, 2 * K1C::STAGE);
  (void)hipFuncSetAttribute((const void*)k2_kernel, hipFuncAttributeMaxDynamicSharedMemorySize, 131072);
  k1_kernel<<<16 * 13, 512, 2 * K1C::STAGE, stream>>>(x, wqkv, w1, b1, ws);
  k2_kernel<<<512, 512, 131072, stream>>>(ws, w2);
  k3_kernel<<<256, 256, 2 * K3C::STAGE, stream>>>(x, wo, b2, ws, out);
}
```

```cpp
#include <hip/hip_runtime.h>
#include <hip/hip_bf16.h>
#include <stdint.h>

typedef __attribute__((ext_vector_type(8))) short bf16x8;
typedef __attribute__((ext_vector_type(4))) short bf16x4;
typedef __attribute__((ext_vector_type(4))) float f32x4;
typedef __attribute__((ext_vector_type(16))) float f32x16;
typedef __attribute__((ext_vector_type(4))) unsigned int u32x4;
typedef __attribute__((ext_vector_type(2))) unsigned int u32x2;
typedef __attribute__((address_space(3))) bf16x4* lds_b4_ptr;
typedef __attribute__((ext_vector_type(2))) __bf16 bf16x2_t;

constexpr int S_ = 2048, D_ = 512, H_ = 32, F_ = 1792;

constexpr size_t WS_Q = 0;
constexpr size_t WS_K = WS_Q + (size_t)H_ * S_ * 16 * 2;
constexpr size_t WS_V = WS_K + (size_t)H_ * S_ * 16 * 2;
constexpr size_t WS_H = WS_V + (size_t)H_ * S_ * 16 * 2;
constexpr size_t WS_Y = WS_H + (size_t)S_ * F_ * 2;
constexpr size_t WS_O = WS_Y + (size_t)S_ * D_ * 4;

__device__ __forceinline__ unsigned short f2bf(float f) {
  __hip_bfloat16 b = __float2bfloat16(f);
  return *reinterpret_cast<unsigned short*>(&b);
}
__device__ __forceinline__ float bf2f(unsigned short u) { return __uint_as_float(((unsigned)u) << 16); }
__device__ __forceinline__ unsigned pack2(float a, float b) { return (unsigned)f2bf(a) | ((unsigned)f2bf(b) << 16); }

template <int BM, int BN, int WM, int WN, int NS_>
struct GemmCfg {
  static constexpr int NT = WM * WN * 64;
  static constexpr int NW = WM * WN;
  static constexpr int NS = NS_;
  static constexpr int A_BYTES = BM * 128;
  static constexpr int B_BYTES = 64 * BN * 2;
  static constexpr int STAGE = A_BYTES + B_BYTES;
  static constexpr int TM = BM / WM / 16;
  static constexpr int TN = BN / WN / 16;
  static constexpr int A_PC = A_BYTES / 1024 / NW;
  static constexpr int B_PC = B_BYTES / 1024 / NW;
  static constexpr int LD_PER_STAGE = A_PC + B_PC;
};
template <int BN>
__device__ __forceinline__ int bswz(int row) { return BN == 64 ? (((row >> 1) & 3) << 1) : ((row & 7) << 1); }

template <int N>
__device__ __forceinline__ void wait_vm() { asm volatile("s_waitcnt vmcnt(%0)" ::"n"(N) : "memory"); }

template <int BM, int BN, int WM, int WN, int NS_, bool SWAP, bool COLSUM>
__device__ __forceinline__ void gemm_mainloop(const unsigned short* __restrict__ A, int lda,
                                              const unsigned short* __restrict__ B, int ldb, int K,
                                              char* smem,
                                              f32x4 (&acc)[GemmCfg<BM, BN, WM, WN, NS_>::TM][GemmCfg<BM, BN, WM, WN, NS_>::TN],
                                              f32x4 (&cs)[GemmCfg<BM, BN, WM, WN, NS_>::TN]) {
  using C = GemmCfg<BM, BN, WM, WN, NS_>;
  const int tid = threadIdx.x, lane = tid & 63, wid = __builtin_amdgcn_readfirstlane(tid >> 6);
  const int wm = wid / WN, wn = wid % WN;

  int aoff[C::A_PC], boff[C::B_PC];
#pragma unroll
  for (int i = 0; i < C::A_PC; ++i) {
    int pc = wid * C::A_PC + i;
    int row = pc * 8 + (lane >> 3), ch = (lane & 7) ^ (row & 7);
    aoff[i] = row * lda + ch * 8;
  }
#pragma unroll
  for (int i = 0; i < C::B_PC; ++i) {
    int pc = wid * C::B_PC + i;
    int byte = pc * 1024 + lane * 16;
    int row = byte / (BN * 2), ch = ((byte % (BN * 2)) >> 4) ^ bswz<BN>(row);
    boff[i] = row * ldb + ch * 8;
  }
  auto issue = [&](int kt) {
    char* base = smem + (kt % C::NS) * C::STAGE;
#pragma unroll
    for (int i = 0; i < C::A_PC; ++i)
      __builtin_amdgcn_global_load_lds((const __attribute__((address_space(1))) void*)(A + aoff[i] + kt * 64),
                                       (__attribute__((address_space(3))) void*)(base + (wid * C::A_PC + i) * 1024), 16, 0, 0);
#pragma unroll
    for (int i = 0; i < C::B_PC; ++i)
      __builtin_amdgcn_global_load_lds((const __attribute__((address_space(1))) void*)(B + boff[i] + (size_t)kt * 64 * ldb),
                                       (__attribute__((address_space(3))) void*)(base + C::A_BYTES + (wid * C::B_PC + i) * 1024), 16, 0, 0);
  };

  const int nk = K / 64;
#pragma unroll
  for (int s = 0; s < C::NS - 1; ++s) issue(s);
  const int g = lane >> 4, q = (lane & 15) >> 2, p = lane & 3;
  bf16x8 ones;
#pragma unroll
  for (int j = 0; j < 8; ++j) ones[j] = (short)0x3F80;

  for (int kt = 0; kt < nk; ++kt) {
    if (kt + C::NS - 2 < nk) wait_vm<(C::NS - 2) * C::LD_PER_STAGE>();
    else wait_vm<0>();
    __builtin_amdgcn_s_barrier();
    if (kt + C::NS - 1 < nk) issue(kt + C::NS - 1);
    const char* base = smem + (kt % C::NS) * C::STAGE;
#pragma unroll
    for (int kk = 0; kk < 2; ++kk) {
      bf16x8 af[C::TM], bf[C::TN];
#pragma unroll
      for (int i = 0; i < C::TM; ++i) {
        int row = wm * (BM / WM) + i * 16 + (lane & 15);
        int ch = kk * 4 + g;
        af[i] = *reinterpret_cast<const bf16x8*>(base + row * 128 + ((ch ^ (row & 7)) << 4));
      }
#pragma unroll
      for (int j = 0; j < C::TN; ++j) {
        int col = wn * (BN / WN) + j * 16 + 4 * p;
        int r0 = kk * 32 + 8 * g + q, r1 = r0 + 4;
        const char* a0 = base + C::A_BYTES + r0 * (BN * 2) + ((((col >> 3) ^ bswz<BN>(r0)) << 4) | ((col & 7) * 2));
        const char* a1 = base + C::A_BYTES + r1 * (BN * 2) + ((((col >> 3) ^ bswz<BN>(r1)) << 4) | ((col & 7) * 2));
        bf16x4 lo = __builtin_amdgcn_ds_read_tr16_b64_v4i16((lds_b4_ptr)(a0));
        bf16x4 hi = __builtin_amdgcn_ds_read_tr16_b64_v4i16((lds_b4_ptr)(a1));
        bf[j] = __builtin_shufflevector(lo, hi, 0, 1, 2, 3, 4, 5, 6, 7);
      }
#pragma unroll
      for (int i = 0; i < C::TM; ++i)
#pragma unroll
        for (int j = 0; j < C::TN; ++j) {
          if (SWAP)
            acc[i][j] = __builtin_amdgcn_mfma_f32_16x16x32_bf16(bf[j], af[i], acc[i][j], 0, 0, 0);
          else
            acc[i][j] = __builtin_amdgcn_mfma_f32_16x16x32_bf16(af[i], bf[j], acc[i][j], 0, 0, 0);
        }
      if (COLSUM) {
        if (((kt * 2 + kk) % WM) == wm) {
#pragma unroll
          for (int j = 0; j < C::TN; ++j) cs[j] = __builtin_amdgcn_mfma_f32_16x16x32_bf16(ones, bf[j], cs[j], 0, 0, 0);
        }
      }
    }
  }
  __syncthreads();
}

__device__ __forceinline__ float gelu_tanh(float x) {
  float u = 0.7978845608028654f * (x + 0.044715f * x * x * x);
  float e = __expf(2.f * u);
  float t = 1.f - 2.f * __builtin_amdgcn_rcpf(e + 1.f);
  return 0.5f * x * (1.f + t);
}

constexpr int K1_BM = 128, K1_BN = 256, K1_WM = 2, K1_WN = 4;
using K1C = GemmCfg<K1_BM, K1_BN, K1_WM, K1_WN, 3>;

__global__ __launch_bounds__(512) void k1_kernel(const unsigned short* __restrict__ x,
                                                 const unsigned short* __restrict__ wqkv,
                                                 const unsigned short* __restrict__ w1,
                                                 const unsigned short* __restrict__ b1, char* __restrict__ ws) {
  extern __shared__ __attribute__((aligned(16))) char smem[];
  __shared__ float s_mean[K1_BM], s_rstd[K1_BM], s_cs[K1_WM][K1_BN];
  const int tid = threadIdx.x, lane = tid & 63, wid = tid >> 6;
  const int wm = wid / K1_WN, wn = wid % K1_WN;
  const int mt = blockIdx.x / 13, nt = blockIdx.x % 13;
  const int m0 = mt * K1_BM;
  const unsigned short* A = x + (size_t)m0 * D_;

  {
    float s[16], ss[16];
#pragma unroll
    for (int r = 0; r < 16; ++r) {
      u32x4 v = *reinterpret_cast<const u32x4*>(A + (size_t)(wid * 16 + r) * D_ + lane * 8);
      float a0 = 0.f, a1 = 0.f;
#pragma unroll
      for (int j = 0; j < 4; ++j) {
        {float a=__uint_as_float(v[j]<<16),b=__uint_as_float(v[j]&0xffff0000u); a0+=a+b; a1+=a*a+b*b;}
      }
      s[r] = a0; ss[r] = a1;
    }
#pragma unroll
    for (int r = 0; r < 8; ++r) {
      bool up = lane & 32;
      float t0 = up ? s[r] : s[r + 8], k0 = up ? s[r + 8] : s[r];
      float t1 = up ? ss[r] : ss[r + 8], k1 = up ? ss[r + 8] : ss[r];
      s[r] = k0 + __shfl_xor(t0, 32); ss[r] = k1 + __shfl_xor(t1, 32);
    }
#pragma unroll
    for (int r = 0; r < 4; ++r) {
      bool up = lane & 16;
      float t0 = up ? s[r] : s[r + 4], k0 = up ? s[r + 4] : s[r];
      float t1 = up ? ss[r] : ss[r + 4], k1 = up ? ss[r + 4] : ss[r];
      s[r] = k0 + __shfl_xor(t0, 16); ss[r] = k1 + __shfl_xor(t1, 16);
    }
#pragma unroll
    for (int r = 0; r < 2; ++r) {
      bool up = lane & 8;
      float t0 = up ? s[r] : s[r + 2], k0 = up ? s[r + 2] : s[r];
      float t1 = up ? ss[r] : ss[r + 2], k1 = up ? ss[r + 2] : ss[r];
      s[r] = k0 + __shfl_xor(t0, 8); ss[r] = k1 + __shfl_xor(t1, 8);
    }
    {
      bool up = lane & 4;
      float t0 = up ? s[0] : s[1], k0 = up ? s[1] : s[0];
      float t1 = up ? ss[0] : ss[1], k1 = up ? ss[1] : ss[0];
      s[0] = k0 + __shfl_xor(t0, 4); ss[0] = k1 + __shfl_xor(t1, 4);
    }
    s[0] += __shfl_xor(s[0], 1); ss[0] += __shfl_xor(ss[0], 1);
    s[0] += __shfl_xor(s[0], 2); ss[0] += __shfl_xor(ss[0], 2);
    int row = wid * 16 + 8 * (lane >> 5) + 4 * ((lane >> 4) & 1) + 2 * ((lane >> 3) & 1) + ((lane >> 2) & 1);
    float mean = s[0] * (1.f / D_);
    float var = fmaxf(ss[0] * (1.f / D_) - mean * mean, 0.f);
    if ((lane & 3) == 0) { s_mean[row] = mean; s_rstd[row] = rsqrtf(var + 1e-5f); }
  }

  f32x4 acc[K1C::TM][K1C::TN];
  f32x4 cs[K1C::TN];
#pragma unroll
  for (int i = 0; i < K1C::TM; ++i)
#pragma unroll
    for (int j = 0; j < K1C::TN; ++j) acc[i][j] = f32x4{0, 0, 0, 0};
#pragma unroll
  for (int j = 0; j < K1C::TN; ++j) cs[j] = f32x4{0, 0, 0, 0};

  const bool is_v = (nt == 4 || nt == 5);
  if (nt < 6) {
    const unsigned short* B = wqkv + nt * K1_BN;
    if (is_v)
      gemm_mainloop<K1_BM, K1_BN, K1_WM, K1_WN, 3, false, true>(A, D_, B, 3 * D_, D_, smem, acc, cs);
    else
      gemm_mainloop<K1_BM, K1_BN, K1_WM, K1_WN, 3, true, true>(A, D_, B, 3 * D_, D_, smem, acc, cs);
  } else {
    const unsigned short* B = w1 + (nt - 6) * K1_BN;
    gemm_mainloop<K1_BM, K1_BN, K1_WM, K1_WN, 3, true, true>(A, D_, B, F_, D_, smem, acc, cs);
  }
  if (lane < 16) {
#pragma unroll
    for (int j = 0; j < K1C::TN; ++j) s_cs[wm][wn * 64 + j * 16 + lane] = cs[j][0];
  }
  __syncthreads();

#pragma unroll
  for (int i = 0; i < K1C::TM; ++i) {
#pragma unroll
    for (int j = 0; j < K1C::TN; ++j) {
      const int nloc = wn * 64 + j * 16;
      const int mloc = wm * 64 + i * 16;
      if (is_v) {
        int n = nloc + (lane & 15);
        float csn = s_cs[0][n] + s_cs[1][n];
        float v[4];
#pragma unroll
        for (int r = 0; r < 4; ++r) {
          int m = mloc + 4 * (lane >> 4) + r;
          v[r] = s_rstd[m] * (acc[i][j][r] - s_mean[m] * csn);
        }
        int head = (nt - 4) * 16 + (nloc >> 4);
        int key = m0 + mloc + 4 * (lane >> 4);
        int grp = key >> 4, kk = key & 15;
        int hh = (kk >> 2) & 1, jb = (kk >> 3) * 4;
        unsigned short* dst = reinterpret_cast<unsigned short*>(ws + WS_V) + (size_t)head * S_ * 16 +
                              ((size_t)(grp * 2 + hh) * 16 + (lane & 15)) * 8 + jb;
        *reinterpret_cast<u32x2*>(dst) = u32x2{pack2(v[0], v[1]), pack2(v[2], v[3])};
      } else {
        int m = mloc + (lane & 15);
        float mean = s_mean[m], rstd = s_rstd[m];
        float v[4];
#pragma unroll
        for (int r = 0; r < 4; ++r) {
          int n = nloc + 4 * (lane >> 4) + r;
          float csn = s_cs[0][n] + s_cs[1][n];
          v[r] = rstd * (acc[i][j][r] - mean * csn);
        }
        if (nt < 4) {
          const bool isq = nt < 2;
          if (isq) {
#pragma unroll
            for (int r = 0; r < 4; ++r) v[r] *= 0.25f * 1.4426950408889634f;
          }
          int head = (nt & 1) * 16 + (nloc >> 4);
          unsigned short* dst = reinterpret_cast<unsigned short*>(ws + (isq ? WS_Q : WS_K)) +
                                ((size_t)head * S_ + m0 + m) * 16 + 4 * (lane >> 4);
          *reinterpret_cast<u32x2*>(dst) = u32x2{pack2(v[0], v[1]), pack2(v[2], v[3])};
        } else {
          int n = (nt - 6) * K1_BN + nloc + 4 * (lane >> 4);
          u32x2 bb = *reinterpret_cast<const u32x2*>(b1 + n);
          v[0] = gelu_tanh(v[0] + __uint_as_float(bb[0] << 16));
          v[1] = gelu_tanh(v[1] + __uint_as_float(bb[0] & 0xffff0000u));
          v[2] = gelu_tanh(v[2] + __uint_as_float(bb[1] << 16));
          v[3] = gelu_tanh(v[3] + __uint_as_float(bb[1] & 0xffff0000u));
          unsigned short* dst = reinterpret_cast<unsigned short*>(ws + WS_H) + (size_t)(m0 + m) * F_ + n;
          *reinterpret_cast<u32x2*>(dst) = u32x2{pack2(v[0], v[1]), pack2(v[2], v[3])};
        }
      }
    }
  }
}

constexpr int F2_BM = 64, F2_BN = 64, F2_WM = 4, F2_WN = 2;
using F2C = GemmCfg<F2_BM, F2_BN, F2_WM, F2_WN, 4>;

__device__ __forceinline__ void pack_p(const f32x16& p, bf16x8& p0, bf16x8& p1) {
  unsigned w[8];
#pragma unroll
  for (int i = 0; i < 8; ++i) {
    asm("v_cvt_pk_bf16_f32 %0, %1, %2" : "=v"(w[i]) : "v"(p[2 * i]), "v"(p[2 * i + 1]));
  }
  p0 = __builtin_bit_cast(bf16x8, u32x4{w[0], w[1], w[2], w[3]});
  p1 = __builtin_bit_cast(bf16x8, u32x4{w[4], w[5], w[6], w[7]});
}

__global__ __launch_bounds__(512) void k2_kernel(char* __restrict__ ws, const unsigned short* __restrict__ w2) {
  extern __shared__ __attribute__((aligned(16))) char smem[];
  const int tid = threadIdx.x, lane = tid & 63, wid = tid >> 6;
  if (blockIdx.x >= 256) {
    const int b = blockIdx.x - 256;
    const int mt = b >> 3, nt = b & 7;
    const int wm = wid / F2_WN, wn = wid % F2_WN;
    f32x4 acc[F2C::TM][F2C::TN];
    f32x4 cs[F2C::TN];
#pragma unroll
    for (int i = 0; i < F2C::TM; ++i)
#pragma unroll
      for (int j = 0; j < F2C::TN; ++j) acc[i][j] = f32x4{0, 0, 0, 0};
    const unsigned short* A = reinterpret_cast<const unsigned short*>(ws + WS_H) + (size_t)mt * F2_BM * F_;
    gemm_mainloop<F2_BM, F2_BN, F2_WM, F2_WN, 4, true, false>(A, F_, w2 + nt * F2_BN, D_, F_, smem, acc, cs);
    float* y = reinterpret_cast<float*>(ws + WS_Y);
#pragma unroll
    for (int i = 0; i < F2C::TM; ++i)
#pragma unroll
      for (int j = 0; j < F2C::TN; ++j) {
        int m = mt * F2_BM + wm * (F2_BM / F2_WM) + i * 16 + (lane & 15);
        int n = nt * F2_BN + wn * (F2_BN / F2_WN) + j * 16 + 4 * (lane >> 4);
        *reinterpret_cast<f32x4*>(y + (size_t)m * D_ + n) = acc[i][j];
      }
    return;
  }
  const int head = blockIdx.x >> 3, qblk = blockIdx.x & 7;
  char* sK = smem;
  char* sV = smem + 65536;
  {
    const u32x4* gK = reinterpret_cast<const u32x4*>(ws + WS_K + (size_t)head * 65536);
    const u32x4* gV = reinterpret_cast<const u32x4*>(ws + WS_V + (size_t)head * 65536);
#pragma unroll
    for (int i = 0; i < 8; ++i) {
      reinterpret_cast<u32x4*>(sK)[tid + i * 512] = gK[tid + i * 512];
      reinterpret_cast<u32x4*>(sV)[tid + i * 512] = gV[tid + i * 512];
    }
  }
  const int q0 = qblk * 256 + wid * 32;
  const int hh = lane >> 5, r31 = lane & 31;
  bf16x8 qf = *reinterpret_cast<const bf16x8*>(ws + WS_Q + ((size_t)head * S_ + q0 + r31) * 32 + hh * 16);
  __syncthreads();

  bf16x8 vconst;
#pragma unroll
  for (int j = 0; j < 8; ++j) vconst[j] = (r31 == 16) ? (short)0x3F80 : (short)0;

  auto kfrag = [&](int tile) { return *reinterpret_cast<const bf16x8*>(sK + (tile * 32 + r31) * 32 + hh * 16); };
  auto vfrag = [&](int grp) {
    bf16x8 v = vconst;
    if (r31 < 16) v = *reinterpret_cast<const bf16x8*>(sV + ((grp * 2 + hh) * 16 + r31) * 16);
    return v;
  };

  f32x16 zero16;
#pragma unroll
  for (int i = 0; i < 16; ++i) zero16[i] = 0.f;
  f32x16 negm;
  {
    f32x16 s = __builtin_amdgcn_mfma_f32_32x32x16_bf16(kfrag(0), qf, zero16, 0, 0, 0);
    float m = s[0];
#pragma unroll
    for (int i = 1; i < 16; ++i) m = fmaxf(m, s[i]);
    m = fmaxf(m, __shfl_xor(m, 32));
#pragma unroll
    for (int i = 0; i < 16; ++i) negm[i] = -m;
  }
  f32x16 o = zero16;
  {
    const char* kp = sK + r31 * 32 + hh * 16;
    const char* vp = sV + (hh * 16 + (r31 & 15)) * 16;
    bf16x8 va0 = vconst, va1 = vconst, vb0 = vconst, vb1 = vconst;
    if (r31 < 16) { va0 = *reinterpret_cast<const bf16x8*>(vp); va1 = *reinterpret_cast<const bf16x8*>(vp + 512); }
    f32x16 sn = __builtin_amdgcn_mfma_f32_32x32x16_bf16(*reinterpret_cast<const bf16x8*>(kp), qf, negm, 0, 0, 0);
    for (int t = 0; t < 64; t += 2) {
      {
        bf16x8 kf = *reinterpret_cast<const bf16x8*>(kp + (t + 1) * 1024);
        if (r31 < 16) {
          vb0 = *reinterpret_cast<const bf16x8*>(vp + (t + 1) * 1024);
          vb1 = *reinterpret_cast<const bf16x8*>(vp + (t + 1) * 1024 + 512);
        }
        f32x16 sc = sn;
#pragma unroll
        for (int i = 0; i < 16; ++i) sc[i] = __builtin_amdgcn_exp2f(sc[i]);
        bf16x8 p0, p1;
        pack_p(sc, p0, p1);
        sn = __builtin_amdgcn_mfma_f32_32x32x16_bf16(kf, qf, negm, 0, 0, 0);
        o = __builtin_amdgcn_mfma_f32_32x32x16_bf16(va0, p0, o, 0, 0, 0);
        o = __builtin_amdgcn_mfma_f32_32x32x16_bf16(va1, p1, o, 0, 0, 0);
      }
      {
        const int tn = (t + 2 < 64) ? t + 2 : 0;
        bf16x8 kf = *reinterpret_cast<const bf16x8*>(kp + tn * 1024);
        if (r31 < 16) {
          va0 = *reinterpret_cast<const bf16x8*>(vp + tn * 1024);
          va1 = *reinterpret_cast<const bf16x8*>(vp + tn * 1024 + 512);
        }
        f32x16 sc = sn;
#pragma unroll
        for (int i = 0; i < 16; ++i) sc[i] = __builtin_amdgcn_exp2f(sc[i]);
        bf16x8 p0, p1;
        pack_p(sc, p0, p1);
        sn = __builtin_amdgcn_mfma_f32_32x32x16_bf16(kf, qf, negm, 0, 0, 0);
        o = __builtin_amdgcn_mfma_f32_32x32x16_bf16(vb0, p0, o, 0, 0, 0);
        o = __builtin_amdgcn_mfma_f32_32x32x16_bf16(vb1, p1, o, 0, 0, 0);
      }
    }
  }
  float l = o[8];
  l = __shfl(l, r31);
  bool bad = !(l < 1.8446744073709552e19f);
  if (__any(bad)) {
    float m = -INFINITY;
    float lacc = 0.f;
    o = zero16;
    for (int t = 0; t < 64; ++t) {
      f32x16 s = __builtin_amdgcn_mfma_f32_32x32x16_bf16(kfrag(t), qf, zero16, 0, 0, 0);
      float tm = s[0];
#pragma unroll
      for (int i = 1; i < 16; ++i) tm = fmaxf(tm, s[i]);
      tm = fmaxf(tm, __shfl_xor(tm, 32));
      float mn = fmaxf(m, tm);
      float alpha = __builtin_amdgcn_exp2f(m - mn);
      m = mn;
#pragma unroll
      for (int i = 0; i < 16; ++i) { s[i] = __builtin_amdgcn_exp2f(s[i] - m); o[i] *= alpha; }
      bf16x8 p0, p1;
      pack_p(s, p0, p1);
      o = __builtin_amdgcn_mfma_f32_32x32x16_bf16(vfrag(2 * t), p0, o, 0, 0, 0);
      o = __builtin_amdgcn_mfma_f32_32x32x16_bf16(vfrag(2 * t + 1), p1, o, 0, 0, 0);
    }
    lacc = o[8];
    l = __shfl(lacc, r31);
  }
  float inv = 1.f / l;
  unsigned short* dst = reinterpret_cast<unsigned short*>(ws + WS_O) + (size_t)(q0 + r31) * D_ + head * 16 + 4 * hh;
  *reinterpret_cast<u32x2*>(dst) = u32x2{pack2(o[0] * inv, o[1] * inv), pack2(o[2] * inv, o[3] * inv)};
  *reinterpret_cast<u32x2*>(dst + 8) = u32x2{pack2(o[4] * inv, o[5] * inv), pack2(o[6] * inv, o[7] * inv)};
}

constexpr int K3_BM = 64, K3_BN = 64, K3_WM = 2, K3_WN = 2;
using K3C = GemmCfg<K3_BM, K3_BN, K3_WM, K3_WN, 4>;

__global__ __launch_bounds__(256) void k3_kernel(const unsigned short* __restrict__ x,
                                                 const unsigned short* __restrict__ wo,
                                                 const unsigned short* __restrict__ b2, const char* __restrict__ ws,
                                                 unsigned short* __restrict__ out) {
  extern __shared__ __attribute__((aligned(16))) char smem[];
  const int tid = threadIdx.x, lane = tid & 63, wid = tid >> 6;
  const int mt = blockIdx.x >> 3, nt = blockIdx.x & 7;
  const int wm = wid / K3_WN, wn = wid % K3_WN;
  f32x4 acc[K3C::TM][K3C::TN];
  f32x4 cs[K3C::TN];
#pragma unroll
  for (int i = 0; i < K3C::TM; ++i)
#pragma unroll
    for (int j = 0; j < K3C::TN; ++j) acc[i][j] = f32x4{0, 0, 0, 0};
  const unsigned short* A = reinterpret_cast<const unsigned short*>(ws + WS_O) + (size_t)mt * K3_BM * D_;
  gemm_mainloop<K3_BM, K3_BN, K3_WM, K3_WN, 4, true, false>(A, D_, wo + nt * K3_BN, D_, D_, smem, acc, cs);
  const float* y = reinterpret_cast<const float*>(ws + WS_Y);
#pragma unroll
  for (int i = 0; i < K3C::TM; ++i)
#pragma unroll
    for (int j = 0; j < K3C::TN; ++j) {
      int m = mt * K3_BM + wm * (K3_BM / K3_WM) + i * 16 + (lane & 15);
      int n = nt * K3_BN + wn * (K3_BN / K3_WN) + j * 16 + 4 * (lane >> 4);
      f32x4 yv = *reinterpret_cast<const f32x4*>(y + (size_t)m * D_ + n);
      u32x2 xv = *reinterpret_cast<const u32x2*>(x + (size_t)m * D_ + n);
      u32x2 bv = *reinterpret_cast<const u32x2*>(b2 + n);
      float r0 = acc[i][j][0] + yv[0] + __uint_as_float(xv[0] << 16) + __uint_as_float(bv[0] << 16);
      float r1 = acc[i][j][1] + yv[1] + __uint_as_float(xv[0] & 0xffff0000u) + __uint_as_float(bv[0] & 0xffff0000u);
      float r2 = acc[i][j][2] + yv[2] + __uint_as_float(xv[1] << 16) + __uint_as_float(bv[1] << 16);
      float r3 = acc[i][j][3] + yv[3] + __uint_as_float(xv[1] & 0xffff0000u) + __uint_as_float(bv[1] & 0xffff0000u);
      *reinterpret_cast<u32x2*>(out + (size_t)m * D_ + n) = u32x2{pack2(r0, r1), pack2(r2, r3)};
    }
}

extern "C" void kernel_launch(void* const* d_in, const int* in_sizes, int n_in, void* d_out, int out_size,
                              void* d_ws, size_t ws_size, hipStream_t stream) {
  const unsigned short* x = (const unsigned short*)d_in[0];
  const unsigned short* wqkv = (const unsigned short*)d_in[1];
  const unsigned short* wo = (const unsigned short*)d_in[2];
  const unsigned short* w1 = (const unsigned short*)d_in[4];
  const unsigned short* b1 = (const unsigned short*)d_in[5];
  const unsigned short* w2 = (const unsigned short*)d_in[6];
  const unsigned short* b2 = (const unsigned short*)d_in[7];
  char* ws = (char*)d_ws;
  unsigned short* out = (unsigned short*)d_out;

  (void)hipFuncSetAttribute((const void*)k1_kernel, hipFuncAttributeMaxDynamicSharedMemorySize, K1C::NS * K1C::STAGE);
  (void)hipFuncSetAttribute((const void*)k2_kernel, hipFuncAttributeMaxDynamicSharedMemorySize, 131072);
  k1_kernel<<<16 * 13, 512, K1C::NS * K1C::STAGE, stream>>>(x, wqkv, w1, b1, ws);
  k2_kernel<<<512, 512, 131072, stream>>>(ws, w2);
  k3_kernel<<<256, 256, K3C::NS * K3C::STAGE, stream>>>(x, wo, b2, ws, out);
}
```

```cpp
#include <hip/hip_runtime.h>
#include <hip/hip_bf16.h>
#include <stdint.h>

typedef __attribute__((ext_vector_type(8))) short bf16x8;
typedef __attribute__((ext_vector_type(4))) short bf16x4;
typedef __attribute__((ext_vector_type(4))) float f32x4;
typedef __attribute__((ext_vector_type(16))) float f32x16;
typedef __attribute__((ext_vector_type(4))) unsigned int u32x4;
typedef __attribute__((ext_vector_type(2))) unsigned int u32x2;
typedef __attribute__((address_space(3))) bf16x4* lds_b4_ptr;
typedef __attribute__((ext_vector_type(2))) __bf16 bf16x2_t;

constexpr int S_ = 2048, D_ = 512, H_ = 32, F_ = 1792;

constexpr size_t WS_Q = 0;
constexpr size_t WS_K = WS_Q + (size_t)H_ * S_ * 16 * 2;
constexpr size_t WS_V = WS_K + (size_t)H_ * S_ * 16 * 2;
constexpr size_t WS_H = WS_V + (size_t)H_ * S_ * 16 * 2;
constexpr size_t WS_Y = WS_H + (size_t)S_ * F_ * 2;
constexpr size_t WS_O = WS_Y + (size_t)S_ * D_ * 4;

__device__ __forceinline__ unsigned short f2bf(float f) {
  __hip_bfloat16 b = __float2bfloat16(f);
  return *reinterpret_cast<unsigned short*>(&b);
}
__device__ __forceinline__ float bf2f(unsigned short u) { return __uint_as_float(((unsigned)u) << 16); }
__device__ __forceinline__ unsigned pack2(float a, float b) { return (unsigned)f2bf(a) | ((unsigned)f2bf(b) << 16); }

template <int BM, int BN, int WM, int WN, int NS_>
struct GemmCfg {
  static constexpr int NT = WM * WN * 64;
  static constexpr int NW = WM * WN;
  static constexpr int NS = NS_;
  static constexpr int A_BYTES = BM * 128;
  static constexpr int B_BYTES = 64 * BN * 2;
  static constexpr int STAGE = A_BYTES + B_BYTES;
  static constexpr int TM = BM / WM / 16;
  static constexpr int TN = BN / WN / 16;
  static constexpr int A_PC = A_BYTES / 1024 / NW;
  static constexpr int B_PC = B_BYTES / 1024 / NW;
  static constexpr int LD_PER_STAGE = A_PC + B_PC;
};
template <int BN>
__device__ __forceinline__ int bswz(int row) { return BN == 64 ? (((row >> 1) & 3) << 1) : ((row & 7) << 1); }

template <int N>
__device__ __forceinline__ void wait_vm() { asm volatile("s_waitcnt vmcnt(%0)" ::"n"(N) : "memory"); }

template <int BM, int BN, int WM, int WN, int NS_, bool SWAP, bool COLSUM>
__device__ __forceinline__ void gemm_mainloop(const unsigned short* __restrict__ A, int lda,
                                              const unsigned short* __restrict__ B, int ldb, int K,
                                              char* smem,
                                              f32x4 (&acc)[GemmCfg<BM, BN, WM, WN, NS_>::TM][GemmCfg<BM, BN, WM, WN, NS_>::TN],
                                              f32x4 (&cs)[GemmCfg<BM, BN, WM, WN, NS_>::TN]) {
  using C = GemmCfg<BM, BN, WM, WN, NS_>;
  const int tid = threadIdx.x, lane = tid & 63, wid = __builtin_amdgcn_readfirstlane(tid >> 6);
  const int wm = wid / WN, wn = wid % WN;

  int aoff[C::A_PC], boff[C::B_PC];
#pragma unroll
  for (int i = 0; i < C::A_PC; ++i) {
    int pc = wid * C::A_PC + i;
    int row = pc * 8 + (lane >> 3), ch = (lane & 7) ^ (row & 7);
    aoff[i] = row * lda + ch * 8;
  }
#pragma unroll
  for (int i = 0; i < C::B_PC; ++i) {
    int pc = wid * C::B_PC + i;
    int byte = pc * 1024 + lane * 16;
    int row = byte / (BN * 2), ch = ((byte % (BN * 2)) >> 4) ^ bswz<BN>(row);
    boff[i] = row * ldb + ch * 8;
  }
  auto issue = [&](int kt) {
    char* base = smem + (kt % C::NS) * C::STAGE;
#pragma unroll
    for (int i = 0; i < C::A_PC; ++i)
      __builtin_amdgcn_global_load_lds((const __attribute__((address_space(1))) void*)(A + aoff[i] + kt * 64),
                                       (__attribute__((address_space(3))) void*)(base + (wid * C::A_PC + i) * 1024), 16, 0, 0);
#pragma unroll
    for (int i = 0; i < C::B_PC; ++i)
      __builtin_amdgcn_global_load_lds((const __attribute__((address_space(1))) void*)(B + boff[i] + (size_t)kt * 64 * ldb),
                                       (__attribute__((address_space(3))) void*)(base + C::A_BYTES + (wid * C::B_PC + i) * 1024), 16, 0, 0);
  };

  const int nk = K / 64;
#pragma unroll
  for (int s = 0; s < C::NS - 1; ++s) issue(s);
  const int g = lane >> 4, q = (lane & 15) >> 2, p = lane & 3;
  bf16x8 ones;
#pragma unroll
  for (int j = 0; j < 8; ++j) ones[j] = (short)0x3F80;

  for (int kt = 0; kt < nk; ++kt) {
    if (kt + C::NS - 2 < nk) wait_vm<(C::NS - 2) * C::LD_PER_STAGE>();
    else wait_vm<0>();
    __builtin_amdgcn_s_barrier();
    if (kt + C::NS - 1 < nk) issue(kt + C::NS - 1);
    const char* base = smem + (kt % C::NS) * C::STAGE;
#pragma unroll
    for (int kk = 0; kk < 2; ++kk) {
      bf16x8 af[C::TM], bf[C::TN];
#pragma unroll
      for (int i = 0; i < C::TM; ++i) {
        int row = wm * (BM / WM) + i * 16 + (lane & 15);
        int ch = kk * 4 + g;
        af[i] = *reinterpret_cast<const bf16x8*>(base + row * 128 + ((ch ^ (row & 7)) << 4));
      }
#pragma unroll
      for (int j = 0; j < C::TN; ++j) {
        int col = wn * (BN / WN) + j * 16 + 4 * p;
        int r0 = kk * 32 + 8 * g + q, r1 = r0 + 4;
        const char* a0 = base + C::A_BYTES + r0 * (BN * 2) + ((((col >> 3) ^ bswz<BN>(r0)) << 4) | ((col & 7) * 2));
        const char* a1 = base + C::A_BYTES + r1 * (BN * 2) + ((((col >> 3) ^ bswz<BN>(r1)) << 4) | ((col & 7) * 2));
        bf16x4 lo = __builtin_amdgcn_ds_read_tr16_b64_v4i16((lds_b4_ptr)(a0));
        bf16x4 hi = __builtin_amdgcn_ds_read_tr16_b64_v4i16((lds_b4_ptr)(a1));
        bf[j] = __builtin_shufflevector(lo, hi, 0, 1, 2, 3, 4, 5, 6, 7);
      }
#pragma unroll
      for (int i = 0; i < C::TM; ++i)
#pragma unroll
        for (int j = 0; j < C::TN; ++j) {
          if (SWAP)
            acc[i][j] = __builtin_amdgcn_mfma_f32_16x16x32_bf16(bf[j], af[i], acc[i][j], 0, 0, 0);
          else
            acc[i][j] = __builtin_amdgcn_mfma_f32_16x16x32_bf16(af[i], bf[j], acc[i][j], 0, 0, 0);
        }
      if (COLSUM) {
        if (((kt * 2 + kk) % WM) == wm) {
#pragma unroll
          for (int j = 0; j < C::TN; ++j) cs[j] = __builtin_amdgcn_mfma_f32_16x16x32_bf16(ones, bf[j], cs[j], 0, 0, 0);
        }
      }
    }
  }
  __syncthreads();
}

__device__ __forceinline__ float gelu_tanh(float x) {
  float u = 0.7978845608028654f * (x + 0.044715f * x * x * x);
  float e = __expf(2.f * u);
  float t = 1.f - 2.f * __builtin_amdgcn_rcpf(e + 1.f);
  return 0.5f * x * (1.f + t);
}

constexpr int K1_BM = 128, K1_BN = 256, K1_WM = 2, K1_WN = 4;
using K1C = GemmCfg<K1_BM, K1_BN, K1_WM, K1_WN, 3>;

__global__ __launch_bounds__(512) void k1_kernel(const unsigned short* __restrict__ x,
                                                 const unsigned short* __restrict__ wqkv,
                                                 const unsigned short* __restrict__ w1,
                                                 const unsigned short* __restrict__ b1, char* __restrict__ ws) {
  extern __shared__ __attribute__((aligned(16))) char smem[];
  __shared__ float s_mean[K1_BM], s_rstd[K1_BM], s_cs[K1_WM][K1_BN];
  const int tid = threadIdx.x, lane = tid & 63, wid = tid >> 6;
  const int wm = wid / K1_WN, wn = wid % K1_WN;
  const int mt = blockIdx.x / 13, nt = blockIdx.x % 13;
  const int m0 = mt * K1_BM;
  const unsigned short* A = x + (size_t)m0 * D_;

  {
    float s[16], ss[16];
#pragma unroll
    for (int r = 0; r < 16; ++r) {
      u32x4 v = *reinterpret_cast<const u32x4*>(A + (size_t)(wid * 16 + r) * D_ + lane * 8);
      float a0 = 0.f, a1 = 0.f;
#pragma unroll
      for (int j = 0; j < 4; ++j) {
        {float a=__uint_as_float(v[j]<<16),b=__uint_as_float(v[j]&0xffff0000u); a0+=a+b; a1+=a*a+b*b;}
      }
      s[r] = a0; ss[r] = a1;
    }
#pragma unroll
    for (int r = 0; r < 8; ++r) {
      bool up = lane & 32;
      float t0 = up ? s[r] : s[r + 8], k0 = up ? s[r + 8] : s[r];
      float t1 = up ? ss[r] : ss[r + 8], k1 = up ? ss[r + 8] : ss[r];
      s[r] = k0 + __shfl_xor(t0, 32); ss[r] = k1 + __shfl_xor(t1, 32);
    }
#pragma unroll
    for (int r = 0; r < 4; ++r) {
      bool up = lane & 16;
      float t0 = up ? s[r] : s[r + 4], k0 = up ? s[r + 4] : s[r];
      float t1 = up ? ss[r] : ss[r + 4], k1 = up ? ss[r + 4] : ss[r];
      s[r] = k0 + __shfl_xor(t0, 16); ss[r] = k1 + __shfl_xor(t1, 16);
    }
#pragma unroll
    for (int r = 0; r < 2; ++r) {
      bool up = lane & 8;
      float t0 = up ? s[r] : s[r + 2], k0 = up ? s[r + 2] : s[r];
      float t1 = up ? ss[r] : ss[r + 2], k1 = up ? ss[r + 2] : ss[r];
      s[r] = k0 + __shfl_xor(t0, 8); ss[r] = k1 + __shfl_xor(t1, 8);
    }
    {
      bool up = lane & 4;
      float t0 = up ? s[0] : s[1], k0 = up ? s[1] : s[0];
      float t1 = up ? ss[0] : ss[1], k1 = up ? ss[1] : ss[0];
      s[0] = k0 + __shfl_xor(t0, 4); ss[0] = k1 + __shfl_xor(t1, 4);
    }
    s[0] += __shfl_xor(s[0], 1); ss[0] += __shfl_xor(ss[0], 1);
    s[0] += __shfl_xor(s[0], 2); ss[0] += __shfl_xor(ss[0], 2);
    int row = wid * 16 + 8 * (lane >> 5) + 4 * ((lane >> 4) & 1) + 2 * ((lane >> 3) & 1) + ((lane >> 2) & 1);
    float mean = s[0] * (1.f / D_);
    float var = fmaxf(ss[0] * (1.f / D_) - mean * mean, 0.f);
    if ((lane & 3) == 0) { s_mean[row] = mean; s_rstd[row] = rsqrtf(var + 1e-5f); }
  }

  f32x4 acc[K1C::TM][K1C::TN];
  f32x4 cs[K1C::TN];
#pragma unroll
  for (int i = 0; i < K1C::TM; ++i)
#pragma unroll
    for (int j = 0; j < K1C::TN; ++j) acc[i][j] = f32x4{0, 0, 0, 0};
#pragma unroll
  for (int j = 0; j < K1C::TN; ++j) cs[j] = f32x4{0, 0, 0, 0};

  const bool is_v = (nt == 4 || nt == 5);
  if (nt < 6) {
    const unsigned short* B = wqkv + nt * K1_BN;
    if (is_v)
      gemm_mainloop<K1_BM, K1_BN, K1_WM, K1_WN, 3, false, true>(A, D_, B, 3 * D_, D_, smem, acc, cs);
    else
      gemm_mainloop<K1_BM, K1_BN, K1_WM, K1_WN, 3, true, true>(A, D_, B, 3 * D_, D_, smem, acc, cs);
  } else {
    const unsigned short* B = w1 + (nt - 6) * K1_BN;
    gemm_mainloop<K1_BM, K1_BN, K1_WM, K1_WN, 3, true, true>(A, D_, B, F_, D_, smem, acc, cs);
  }
  if (lane < 16) {
#pragma unroll
    for (int j = 0; j < K1C::TN; ++j) s_cs[wm][wn * 64 + j * 16 + lane] = cs[j][0];
  }
  __syncthreads();

#pragma unroll
  for (int i = 0; i < K1C::TM; ++i) {
#pragma unroll
    for (int j = 0; j < K1C::TN; ++j) {
      const int nloc = wn * 64 + j * 16;
      const int mloc = wm * 64 + i * 16;
      if (is_v) {
        int n = nloc + (lane & 15);
        float csn = s_cs[0][n] + s_cs[1][n];
        float v[4];
#pragma unroll
        for (int r = 0; r < 4; ++r) {
          int m = mloc + 4 * (lane >> 4) + r;
          v[r] = s_rstd[m] * (acc[i][j][r] - s_mean[m] * csn);
        }
        int head = (nt - 4) * 16 + (nloc >> 4);
        int key = m0 + mloc + 4 * (lane >> 4);
        int grp = key >> 4, kk = key & 15;
        int hh = (kk >> 2) & 1, jb = (kk >> 3) * 4;
        unsigned short* dst = reinterpret_cast<unsigned short*>(ws + WS_V) + (size_t)head * S_ * 16 +
                              ((size_t)(grp * 2 + hh) * 16 + (lane & 15)) * 8 + jb;
        *reinterpret_cast<u32x2*>(dst) = u32x2{pack2(v[0], v[1]), pack2(v[2], v[3])};
      } else {
        int m = mloc + (lane & 15);
        float mean = s_mean[m], rstd = s_rstd[m];
        float v[4];
#pragma unroll
        for (int r = 0; r < 4; ++r) {
          int n = nloc + 4 * (lane >> 4) + r;
          float csn = s_cs[0][n] + s_cs[1][n];
          v[r] = rstd * (acc[i][j][r] - mean * csn);
        }
        if (nt < 4) {
          const bool isq = nt < 2;
          if (isq) {
#pragma unroll
            for (int r = 0; r < 4; ++r) v[r] *= 0.25f * 1.4426950408889634f;
          }
          int head = (nt & 1) * 16 + (nloc >> 4);
          unsigned short* dst = reinterpret_cast<unsigned short*>(ws + (isq ? WS_Q : WS_K)) +
                                ((size_t)head * S_ + m0 + m) * 16 + 4 * (lane >> 4);
          *reinterpret_cast<u32x2*>(dst) = u32x2{pack2(v[0], v[1]), pack2(v[2], v[3])};
        } else {
          int n = (nt - 6) * K1_BN + nloc + 4 * (lane >> 4);
          u32x2 bb = *reinterpret_cast<const u32x2*>(b1 + n);
          v[0] = gelu_tanh(v[0] + __uint_as_float(bb[0] << 16));
          v[1] = gelu_tanh(v[1] + __uint_as_float(bb[0] & 0xffff0000u));
          v[2] = gelu_tanh(v[2] + __uint_as_float(bb[1] << 16));
          v[3] = gelu_tanh(v[3] + __uint_as_float(bb[1] & 0xffff0000u));
          unsigned short* dst = reinterpret_cast<unsigned short*>(ws + WS_H) + (size_t)(m0 + m) * F_ + n;
          *reinterpret_cast<u32x2*>(dst) = u32x2{pack2(v[0], v[1]), pack2(v[2], v[3])};
        }
      }
    }
  }
}

constexpr int F2_BM = 64, F2_BN = 64, F2_WM = 4, F2_WN = 2;
using F2C = GemmCfg<F2_BM, F2_BN, F2_WM, F2_WN, 4>;

__device__ __forceinline__ void pack_p(const f32x16& p, bf16x8& p0, bf16x8& p1) {
  unsigned w[8];
#pragma unroll
  for (int i = 0; i < 8; ++i) {
    asm("v_cvt_pk_bf16_f32 %0, %1, %2" : "=v"(w[i]) : "v"(p[2 * i]), "v"(p[2 * i + 1]));
  }
  p0 = __builtin_bit_cast(bf16x8, u32x4{w[0], w[1], w[2], w[3]});
  p1 = __builtin_bit_cast(bf16x8, u32x4{w[4], w[5], w[6], w[7]});
}

__global__ __launch_bounds__(512) void k2_kernel(char* __restrict__ ws, const unsigned short* __restrict__ w2) {
  extern __shared__ __attribute__((aligned(16))) char smem[];
  const int tid = threadIdx.x, lane = tid & 63, wid = tid >> 6;
  if (blockIdx.x >= 256) {
    const int b = blockIdx.x - 256;
    const int mt = b >> 3, nt = b & 7;
    const int wm = wid / F2_WN, wn = wid % F2_WN;
    f32x4 acc[F2C::TM][F2C::TN];
    f32x4 cs[F2C::TN];
#pragma unroll
    for (int i = 0; i < F2C::TM; ++i)
#pragma unroll
      for (int j = 0; j < F2C::TN; ++j) acc[i][j] = f32x4{0, 0, 0, 0};
    const unsigned short* A = reinterpret_cast<const unsigned short*>(ws + WS_H) + (size_t)mt * F2_BM * F_;
    gemm_mainloop<F2_BM, F2_BN, F2_WM, F2_WN, 4, true, false>(A, F_, w2 + nt * F2_BN, D_, F_, smem, acc, cs);
    float* y = reinterpret_cast<float*>(ws + WS_Y);
#pragma unroll
    for (int i = 0; i < F2C::TM; ++i)
#pragma unroll
      for (int j = 0; j < F2C::TN; ++j) {
        int m = mt * F2_BM + wm * (F2_BM / F2_WM) + i * 16 + (lane & 15);
        int n = nt * F2_BN + wn * (F2_BN / F2_WN) + j * 16 + 4 * (lane >> 4);
        *reinterpret_cast<f32x4*>(y + (size_t)m * D_ + n) = acc[i][j];
      }
    return;
  }
  const int head = blockIdx.x >> 3, qblk = blockIdx.x & 7;
  char* sK = smem;
  char* sV = smem + 32768;
  auto load_half = [&](int hf) {
    const u32x4* gK = reinterpret_cast<const u32x4*>(ws + WS_K + (size_t)head * 65536 + hf * 32768);
    const u32x4* gV = reinterpret_cast<const u32x4*>(ws + WS_V + (size_t)head * 65536 + hf * 32768);
    u32x4 rk[4], rv[4];
#pragma unroll
    for (int i = 0; i < 4; ++i) { rk[i] = gK[tid + i * 512]; rv[i] = gV[tid + i * 512]; }
    __syncthreads();
#pragma unroll
    for (int i = 0; i < 4; ++i) {
      reinterpret_cast<u32x4*>(sK)[tid + i * 512] = rk[i];
      reinterpret_cast<u32x4*>(sV)[tid + i * 512] = rv[i];
    }
    __syncthreads();
  };
  load_half(0);
  const int q0 = qblk * 256 + wid * 32;
  const int hh = lane >> 5, r31 = lane & 31;
  bf16x8 qf = *reinterpret_cast<const bf16x8*>(ws + WS_Q + ((size_t)head * S_ + q0 + r31) * 32 + hh * 16);

  bf16x8 vconst;
#pragma unroll
  for (int j = 0; j < 8; ++j) vconst[j] = (r31 == 16) ? (short)0x3F80 : (short)0;

  auto kfrag = [&](int tile) { return *reinterpret_cast<const bf16x8*>(sK + (tile * 32 + r31) * 32 + hh * 16); };
  auto vfrag = [&](int grp) {
    bf16x8 v = vconst;
    if (r31 < 16) v = *reinterpret_cast<const bf16x8*>(sV + ((grp * 2 + hh) * 16 + r31) * 16);
    return v;
  };

  f32x16 zero16;
#pragma unroll
  for (int i = 0; i < 16; ++i) zero16[i] = 0.f;
  f32x16 negm;
  {
    f32x16 s = __builtin_amdgcn_mfma_f32_32x32x16_bf16(kfrag(0), qf, zero16, 0, 0, 0);
    float m = s[0];
#pragma unroll
    for (int i = 1; i < 16; ++i) m = fmaxf(m, s[i]);
    m = fmaxf(m, __shfl_xor(m, 32));
#pragma unroll
    for (int i = 0; i < 16; ++i) negm[i] = -m;
  }
  f32x16 o = zero16;
  for (int hf = 0; hf < 2; ++hf) {
    if (hf) load_half(1);
    const char* kp = sK + r31 * 32 + hh * 16;
    const char* vp = sV + (hh * 16 + (r31 & 15)) * 16;
    bf16x8 va0 = vconst, va1 = vconst, vb0 = vconst, vb1 = vconst;
    if (r31 < 16) { va0 = *reinterpret_cast<const bf16x8*>(vp); va1 = *reinterpret_cast<const bf16x8*>(vp + 512); }
    f32x16 sn = __builtin_amdgcn_mfma_f32_32x32x16_bf16(*reinterpret_cast<const bf16x8*>(kp), qf, negm, 0, 0, 0);
    for (int t = 0; t < 32; t += 2) {
      {
        bf16x8 kf = *reinterpret_cast<const bf16x8*>(kp + (t + 1) * 1024);
        if (r31 < 16) {
          vb0 = *reinterpret_cast<const bf16x8*>(vp + (t + 1) * 1024);
          vb1 = *reinterpret_cast<const bf16x8*>(vp + (t + 1) * 1024 + 512);
        }
        f32x16 sc = sn;
#pragma unroll
        for (int i = 0; i < 16; ++i) sc[i] = __builtin_amdgcn_exp2f(sc[i]);
        bf16x8 p0, p1;
        pack_p(sc, p0, p1);
        sn = __builtin_amdgcn_mfma_f32_32x32x16_bf16(kf, qf, negm, 0, 0, 0);
        o = __builtin_amdgcn_mfma_f32_32x32x16_bf16(va0, p0, o, 0, 0, 0);
        o = __builtin_amdgcn_mfma_f32_32x32x16_bf16(va1, p1, o, 0, 0, 0);
      }
      {
        const int tn = (t + 2 < 32) ? t + 2 : 0;
        bf16x8 kf = *reinterpret_cast<const bf16x8*>(kp + tn * 1024);
        if (r31 < 16) {
          va0 = *reinterpret_cast<const bf16x8*>(vp + tn * 1024);
          va1 = *reinterpret_cast<const bf16x8*>(vp + tn * 1024 + 512);
        }
        f32x16 sc = sn;
#pragma unroll
        for (int i = 0; i < 16; ++i) sc[i] = __builtin_amdgcn_exp2f(sc[i]);
        bf16x8 p0, p1;
        pack_p(sc, p0, p1);
        sn = __builtin_amdgcn_mfma_f32_32x32x16_bf16(kf, qf, negm, 0, 0, 0);
        o = __builtin_amdgcn_mfma_f32_32x32x16_bf16(vb0, p0, o, 0, 0, 0);
        o = __builtin_amdgcn_mfma_f32_32x32x16_bf16(vb1, p1, o, 0, 0, 0);
      }
    }
  }
  float l = o[8];
  l = __shfl(l, r31);
  bool bad = !(l < 1.8446744073709552e19f);
  if (__syncthreads_or(bad)) {
    float m = -INFINITY;
    float lacc = 0.f;
    o = zero16;
    for (int t2 = 0; t2 < 64; ++t2) {
      const int t = t2 & 31;
      if (t == 0) load_half(t2 >> 5);
      f32x16 s = __builtin_amdgcn_mfma_f32_32x32x16_bf16(kfrag(t), qf, zero16, 0, 0, 0);
      float tm = s[0];
#pragma unroll
      for (int i = 1; i < 16; ++i) tm = fmaxf(tm, s[i]);
      tm = fmaxf(tm, __shfl_xor(tm, 32));
      float mn = fmaxf(m, tm);
      float alpha = __builtin_amdgcn_exp2f(m - mn);
      m = mn;
#pragma unroll
      for (int i = 0; i < 16; ++i) { s[i] = __builtin_amdgcn_exp2f(s[i] - m); o[i] *= alpha; }
      bf16x8 p0, p1;
      pack_p(s, p0, p1);
      o = __builtin_amdgcn_mfma_f32_32x32x16_bf16(vfrag(2 * t), p0, o, 0, 0, 0);
      o = __builtin_amdgcn_mfma_f32_32x32x16_bf16(vfrag(2 * t + 1), p1, o, 0, 0, 0);
    }
    lacc = o[8];
    l = __shfl(lacc, r31);
  }
  float inv = 1.f / l;
  unsigned short* dst = reinterpret_cast<unsigned short*>(ws + WS_O) + (size_t)(q0 + r31) * D_ + head * 16 + 4 * hh;
  *reinterpret_cast<u32x2*>(dst) = u32x2{pack2(o[0] * inv, o[1] * inv), pack2(o[2] * inv, o[3] * inv)};
  *reinterpret_cast<u32x2*>(dst + 8) = u32x2{pack2(o[4] * inv, o[5] * inv), pack2(o[6] * inv, o[7] * inv)};
}

constexpr int K3_BM = 64, K3_BN = 64, K3_WM = 2, K3_WN = 2;
using K3C = GemmCfg<K3_BM, K3_BN, K3_WM, K3_WN, 4>;

__global__ __launch_bounds__(256) void k3_kernel(const unsigned short* __restrict__ x,
                                                 const unsigned short* __restrict__ wo,
                                                 const unsigned short* __restrict__ b2, const char* __restrict__ ws,
                                                 unsigned short* __restrict__ out) {
  extern __shared__ __attribute__((aligned(16))) char smem[];
  const int tid = threadIdx.x, lane = tid & 63, wid = tid >> 6;
  const int mt = blockIdx.x >> 3, nt = blockIdx.x & 7;
  const int wm = wid / K3_WN, wn = wid % K3_WN;
  f32x4 acc[K3C::TM][K3C::TN];
  f32x4 cs[K3C::TN];
#pragma unroll
  for (int i = 0; i < K3C::TM; ++i)
#pragma unroll
    for (int j = 0; j < K3C::TN; ++j) acc[i][j] = f32x4{0, 0, 0, 0};
  const unsigned short* A = reinterpret_cast<const unsigned short*>(ws + WS_O) + (size_t)mt * K3_BM * D_;
  gemm_mainloop<K3_BM, K3_BN, K3_WM, K3_WN, 4, true, false>(A, D_, wo + nt * K3_BN, D_, D_, smem, acc, cs);
  const float* y = reinterpret_cast<const float*>(ws + WS_Y);
#pragma unroll
  for (int i = 0; i < K3C::TM; ++i)
#pragma unroll
    for (int j = 0; j < K3C::TN; ++j) {
      int m = mt * K3_BM + wm * (K3_BM / K3_WM) + i * 16 + (lane & 15);
      int n = nt * K3_BN + wn * (K3_BN / K3_WN) + j * 16 + 4 * (lane >> 4);
      f32x4 yv = *reinterpret_cast<const f32x4*>(y + (size_t)m * D_ + n);
      u32x2 xv = *reinterpret_cast<const u32x2*>(x + (size_t)m * D_ + n);
      u32x2 bv = *reinterpret_cast<const u32x2*>(b2 + n);
      float r0 = acc[i][j][0] + yv[0] + __uint_as_float(xv[0] << 16) + __uint_as_float(bv[0] << 16);
      float r1 = acc[i][j][1] + yv[1] + __uint_as_float(xv[0] & 0xffff0000u) + __uint_as_float(bv[0] & 0xffff0000u);
      float r2 = acc[i][j][2] + yv[2] + __uint_as_float(xv[1] << 16) + __uint_as_float(bv[1] << 16);
      float r3 = acc[i][j][3] + yv[3] + __uint_as_float(xv[1] & 0xffff0000u) + __uint_as_float(bv[1] & 0xffff0000u);
      *reinterpret_cast<u32x2*>(out + (size_t)m * D_ + n) = u32x2{pack2(r0, r1), pack2(r2, r3)};
    }
}

extern "C" void kernel_launch(void* const* d_in, const int* in_sizes, int n_in, void* d_out, int out_size,
                              void* d_ws, size_t ws_size, hipStream_t stream) {
  const unsigned short* x = (const unsigned short*)d_in[0];
  const unsigned short* wqkv = (const unsigned short*)d_in[1];
  const unsigned short* wo = (const unsigned short*)d_in[2];
  const unsigned short* w1 = (const unsigned short*)d_in[4];
  const unsigned short* b1 = (const unsigned short*)d_in[5];
  const unsigned short* w2 = (const unsigned short*)d_in[6];
  const unsigned short* b2 = (const unsigned short*)d_in[7];
  char* ws = (char*)d_ws;
  unsigned short* out = (unsigned short*)d_out;

  (void)hipFuncSetAttribute((const void*)k1_kernel, hipFuncAttributeMaxDynamicSharedMemorySize, K1C::NS * K1C::STAGE);
  (void)hipFuncSetAttribute((const void*)k2_kernel, hipFuncAttributeMaxDynamicSharedMemorySize, 65536);
  k1_kernel<<<16 * 13, 512, K1C::NS * K1C::STAGE, stream>>>(x, wqkv, w1, b1, ws);
  k2_kernel<<<512, 512, 65536, stream>>>(ws, w2);
  k3_kernel<<<256, 256, K3C::NS * K3C::STAGE, stream>>>(x, wo, b2, ws, out);
}
```
